```python
import torch

def _softmax(x, axis=-1):
    return torch.softmax(x, dim=axis)

def _gelu(x):
    return torch.nn.functional.gelu(x, approximate='tanh')

def _silu(x):
    return torch.nn.functional.silu(x)

def _sigmoid(x):
    return torch.sigmoid(x)

def _layernorm(x, axis=-1, eps=1e-05):
    m = x.mean(dim=axis, keepdim=True)
    v = x.var(dim=axis, keepdim=True, unbiased=False)
    return (x - m) / torch.sqrt(v + eps)

def _scatter_add(tgt, idx, src, axis=0):
    return tgt.clone().index_add_(axis, idx, src)

def _segment_sum(src, seg, n):
    out = torch.zeros((n,) + src.shape[1:], dtype=src.dtype)
    return out.index_add_(0, seg, src)

def _pad(x, pw):
    flat = tuple((v for pair in reversed(pw) for v in pair))
    return torch.nn.functional.pad(x, flat)
kernel_name = 'standalone__kv_cache_quant_pre_rope_vs_post_rope__decode'

def setup_inputs():

    _w = lambda a: (lambda t: t.to(torch.bfloat16) if t.is_floating_point() else t)(torch.as_tensor(a))
    B, Sq, Skv = (2, 1, 4096)
    H, Dh0 = (32, 192)
    G = 128 if 128 > 0 else Dh0
    G = max(2, G // 2 * 2)
    Dh = max(G, Dh0 // G * G)
    Skv = max(Skv, Sq)
    ng = Dh // G
    bits = 8 if 8 > 1 else 4
    bits = max(2, min(8, bits))
    qmax = (1 << bits) - 1
    half = Dh // 2
    inv = 1.0 / 10000.0 ** (torch.arange(half) / max(1, half))
    ang_k = torch.arange(Skv)[:, None] * inv[None, :]
    ang_q = torch.arange(Skv - Sq, Skv)[:, None] * inv[None, :]
    kc = torch.cat([torch.cos(ang_k), torch.cos(ang_k)], dim=-1)
    ks = torch.cat([torch.sin(ang_k), torch.sin(ang_k)], dim=-1)
    qc = torch.cat([torch.cos(ang_q), torch.cos(ang_q)], dim=-1)
    qs = torch.cat([torch.sin(ang_q), torch.sin(ang_q)], dim=-1)
    qk_i = torch.randint(0, qmax + 1, (B, H, Skv, Dh)).to(torch.int32)
    qv_i = torch.randint(0, qmax + 1, (B, H, Skv, Dh)).to(torch.int32)
    ksc = torch.abs((lambda *s: torch.randn(*s, dtype=torch.bfloat16) * 0.05)(B, H, Skv, ng)) * 0.1 + 0.01
    kzp = torch.full((B, H, Skv, ng), qmax / 2.0)
    vsc = torch.abs((lambda *s: torch.randn(*s, dtype=torch.bfloat16) * 0.05)(B, H, Skv, ng)) * 0.1 + 0.01
    vzp = torch.full((B, H, Skv, ng), qmax / 2.0)
    return {'q': _w((lambda *s: torch.randn(*s, dtype=torch.bfloat16) * 0.8)(B, H, Sq, Dh) * 0.5), 'qk': _w(qk_i), 'k_scales': _w(ksc), 'k_zeros': _w(kzp), 'qv': _w(qv_i), 'v_scales': _w(vsc), 'v_zeros': _w(vzp), 'q_cos': _w(qc), 'q_sin': _w(qs), 'k_cos': _w(kc), 'k_sin': _w(ks)}

def reference(q, qk, k_scales, k_zeros, qv, v_scales, v_zeros, q_cos, q_sin, k_cos, k_sin):
    B, H, Sq, Dh = q.shape
    ng = k_scales.shape[-1]
    G = Dh // ng
    half = Dh // 2
    ks = (lambda x, n, axis: torch.repeat_interleave(x, n, dim=axis))(k_scales, G, axis=-1)
    kz = (lambda x, n, axis: torch.repeat_interleave(x, n, dim=axis))(k_zeros, G, axis=-1)
    k = ((lambda x, dt: x.to(dt))(qk, q.dtype) - kz) * ks
    vs = (lambda x, n, axis: torch.repeat_interleave(x, n, dim=axis))(v_scales, G, axis=-1)
    vz = (lambda x, n, axis: torch.repeat_interleave(x, n, dim=axis))(v_zeros, G, axis=-1)
    v = ((lambda x, dt: x.to(dt))(qv, q.dtype) - vz) * vs
    k1 = k[..., :half].clone()
    k2 = k[..., half:].clone()
    krh = torch.concatenate([-k2, k1], axis=-1)
    kr = k * k_cos[None, None, :, :] + krh * k_sin[None, None, :, :]
    q1 = q[..., :half].clone()
    q2 = q[..., half:].clone()
    qrh = torch.concatenate([-q2, q1], axis=-1)
    qr = q * q_cos[None, None, :, :] + qrh * q_sin[None, None, :, :]
    logits = torch.einsum('bhqd,bhkd->bhqk', qr, kr) * float(Dh) ** (-0.5)
    attn = _softmax(logits, axis=-1)
    out = torch.einsum('bhqk,bhkd->bhqd', attn, v)
    return out
```

```cpp
#include <hip/hip_runtime.h>
#include <hip/hip_bf16.h>
#include <stdint.h>

#define KV_AUX 2
#define CS_AUX 0
#define SC_AUX 2
#define ROTN 0
#define ROTM 1

namespace {

constexpr int BH = 64;
constexpr int S = 4096;
constexpr int D = 128;
constexpr int WAVES = 4;
constexpr int NTHR = WAVES * 64;
constexpr int WPH = 8;
constexpr int TILE = 8;
constexpr int SUPER = WPH * WAVES * TILE;
constexpr int NT = S / WPH / TILE;
constexpr int NSTEP = NT / WAVES;
constexpr int REC = D + 2;
constexpr size_t WS_EPOCH = 0;
constexpr size_t WS_GRAN = 64 * 1024;
constexpr int GREC = 136;

typedef int i32x4 __attribute__((ext_vector_type(4)));
typedef unsigned u32x2 __attribute__((ext_vector_type(2)));
typedef unsigned u32x4 __attribute__((ext_vector_type(4)));
typedef float f32x4 __attribute__((ext_vector_type(4)));
typedef float f32x2 __attribute__((ext_vector_type(2)));
typedef __attribute__((address_space(1))) unsigned long long gu64;
typedef __attribute__((address_space(1))) unsigned gu32;

__device__ __forceinline__ float bflo(unsigned x) { return __uint_as_float(x << 16); }
__device__ __forceinline__ float bfhi(unsigned x) { return __uint_as_float(x & 0xffff0000u); }

template <int CTRL>
__device__ __forceinline__ float dpp(float x) {
  return __builtin_bit_cast(float, __builtin_amdgcn_mov_dpp(__builtin_bit_cast(int, x), CTRL, 0xf, 0xf, true));
}
constexpr int XOR1 = 0xB1, XOR2 = 0x4E, ROR4 = 0x124, ROR8 = 0x128;

__device__ __forceinline__ float x16_sum(float x) {
  auto s = __builtin_amdgcn_permlane16_swap(__float_as_uint(x), __float_as_uint(x), false, false);
  return __uint_as_float(s[0]) + __uint_as_float(s[1]);
}

struct Tile {
  i32x4 k[4];
  i32x4 v[4];
  u32x2 c[4];
  u32x2 s[4];
};

__global__ void __launch_bounds__(NTHR, 2)
attn_decode(const unsigned short* __restrict__ q, const int* __restrict__ qk, const unsigned short* __restrict__ ksc,
            const unsigned short* __restrict__ kzp, const int* __restrict__ qv, const unsigned short* __restrict__ vsc,
            const unsigned short* __restrict__ vzp, const unsigned short* __restrict__ qcos,
            const unsigned short* __restrict__ qsin, const unsigned short* __restrict__ kcos,
            const unsigned short* __restrict__ ksin, unsigned char* __restrict__ ws,
            __hip_bfloat16* __restrict__ out) {
  __shared__ __attribute__((aligned(16))) float lds[NT * 32 + WAVES * 2 * (D + 4)];
  float* l_mrg = lds + NT * 32;

#define STAMP_END() do {} while (0)
  const int bh = blockIdx.x / WPH, mem = blockIdx.x % WPH;
  const int tid = threadIdx.x, lane = tid & 63, wave = __builtin_amdgcn_readfirstlane(tid >> 6);
  const int half = lane >> 5, c = lane & 31;
  const int rot = (bh * ROTM + (bh >> 4) * ROTN) & 15;

  const auto rk = __builtin_amdgcn_make_buffer_rsrc((void*)(qk + (size_t)bh * S * D), 0, S * D * 4, 0x00020000);
  const auto rv = __builtin_amdgcn_make_buffer_rsrc((void*)(qv + (size_t)bh * S * D), 0, S * D * 4, 0x00020000);
  const auto rc = __builtin_amdgcn_make_buffer_rsrc((void*)kcos, 0, S * D * 2, 0x00020000);
  const auto rs = __builtin_amdgcn_make_buffer_rsrc((void*)ksin, 0, S * D * 2, 0x00020000);
  const int dA = 4 * c, dB = (4 * c + 64) & 127;
  const unsigned voK = half * 512 + c * 16;
  const unsigned voC = half * 256 + dA * 2;
  const unsigned voS = half * 256 + dB * 2;

  auto load = [&](Tile& t, int st) {
    const int row0 = ((st + rot) & 15) * SUPER + mem * 32 + wave * TILE;
#pragma unroll
    for (int i = 0; i < 4; ++i)
      t.k[i] = __builtin_bit_cast(i32x4, __builtin_amdgcn_raw_buffer_load_b128(rk, voK + i * 1024, row0 * 512, KV_AUX));
#pragma unroll
    for (int i = 0; i < 4; ++i) {
      t.c[i] = __builtin_bit_cast(u32x2, __builtin_amdgcn_raw_buffer_load_b64(rc, voC + i * 512, row0 * 256, CS_AUX));
      t.s[i] = __builtin_bit_cast(u32x2, __builtin_amdgcn_raw_buffer_load_b64(rs, voS + i * 512, row0 * 256, CS_AUX));
    }
#pragma unroll
    for (int i = 0; i < 4; ++i)
      t.v[i] = __builtin_bit_cast(i32x4, __builtin_amdgcn_raw_buffer_load_b128(rv, voK + i * 1024, row0 * 512, KV_AUX));
  };

  Tile A, B;
  load(A, 0);
  __builtin_amdgcn_sched_barrier(0);

  u32x4 scl;
  {
    const unsigned short* s01 = (wave & 1) ? vzp : kzp;
    const unsigned short* s23 = (wave & 1) ? vsc : ksc;
    const unsigned short* sp = (wave & 2) ? s23 : s01;
    const auto rsc = __builtin_amdgcn_make_buffer_rsrc((void*)(sp + (size_t)bh * S), 0, S * 2, 0x00020000);
    const int row = (((lane >> 2) + rot) & 15) * SUPER + mem * 32 + (lane & 3) * TILE;
    scl = __builtin_bit_cast(u32x4, __builtin_amdgcn_raw_buffer_load_b128(rsc, row * 2, 0, SC_AUX));
  }
  const auto rq = __builtin_amdgcn_make_buffer_rsrc((void*)(q + bh * D), 0, D * 2, 0x00020000);
  const auto rqc = __builtin_amdgcn_make_buffer_rsrc((void*)qcos, 0, D * 2, 0x00020000);
  const auto rqs = __builtin_amdgcn_make_buffer_rsrc((void*)qsin, 0, D * 2, 0x00020000);
  const u32x2 qA = __builtin_bit_cast(u32x2, __builtin_amdgcn_raw_buffer_load_b64(rq, dA * 2, 0, 0));
  const u32x2 qB = __builtin_bit_cast(u32x2, __builtin_amdgcn_raw_buffer_load_b64(rq, dB * 2, 0, 0));
  const u32x2 cA = __builtin_bit_cast(u32x2, __builtin_amdgcn_raw_buffer_load_b64(rqc, dA * 2, 0, 0));
  const u32x2 cB = __builtin_bit_cast(u32x2, __builtin_amdgcn_raw_buffer_load_b64(rqc, dB * 2, 0, 0));
  const u32x2 sA = __builtin_bit_cast(u32x2, __builtin_amdgcn_raw_buffer_load_b64(rqs, dA * 2, 0, 0));
  const u32x2 sB = __builtin_bit_cast(u32x2, __builtin_amdgcn_raw_buffer_load_b64(rqs, dB * 2, 0, 0));
  __builtin_amdgcn_sched_barrier(0);
  load(B, 1);
  __builtin_amdgcn_sched_barrier(0);
  gu32* epoch_p = (gu32*)(ws + WS_EPOCH) + bh * 64;
  const unsigned tag = __hip_atomic_load(epoch_p, __ATOMIC_RELAXED, __HIP_MEMORY_SCOPE_AGENT) + 1u;

  {
    constexpr float SC = 0.088388347648318440f * 1.4426950408889634f;
    float* rec = lds + lane * 32 + wave * 8;
    const float mul = wave == 2 ? SC : 1.f;
    const float f[8] = {bflo(scl[0]) * mul, bfhi(scl[0]) * mul, bflo(scl[1]) * mul, bfhi(scl[1]) * mul,
                        bflo(scl[2]) * mul, bfhi(scl[2]) * mul, bflo(scl[3]) * mul, bfhi(scl[3]) * mul};
    const bool z = wave < 2;
    *reinterpret_cast<f32x4*>(rec) = f32x4{f[0], z ? f[2] : f[1], z ? f[4] : f[2], z ? f[6] : f[3]};
    *reinterpret_cast<f32x4*>(rec + 4) = f32x4{z ? f[1] : f[4], z ? f[3] : f[5], z ? f[5] : f[6], f[7]};
  }
  float qa[4], qb[4];
  {
    const float sgnA = (c < 16) ? -1.f : 1.f;
    const float q_a[4] = {bflo(qA[0]), bfhi(qA[0]), bflo(qA[1]), bfhi(qA[1])};
    const float q_b[4] = {bflo(qB[0]), bfhi(qB[0]), bflo(qB[1]), bfhi(qB[1])};
    const float c_a[4] = {bflo(cA[0]), bfhi(cA[0]), bflo(cA[1]), bfhi(cA[1])};
    const float c_b[4] = {bflo(cB[0]), bfhi(cB[0]), bflo(cB[1]), bfhi(cB[1])};
    const float s_a[4] = {bflo(sA[0]), bfhi(sA[0]), bflo(sA[1]), bfhi(sA[1])};
    const float s_b[4] = {bflo(sB[0]), bfhi(sB[0]), bflo(sB[1]), bfhi(sB[1])};
#pragma unroll
    for (int j = 0; j < 4; ++j) {
      const float qrA = q_a[j] * c_a[j] + sgnA * q_b[j] * s_a[j];
      const float qrB = q_b[j] * c_b[j] - sgnA * q_a[j] * s_b[j];
      qa[j] = qrA;
      qb[j] = -sgnA * qrB;
    }
  }
  __syncthreads();

  float m = -INFINITY, l = 0.f;
  float acc[4] = {0.f, 0.f, 0.f, 0.f};

  auto compute = [&](const Tile& t, int st) {
    const float* rec = lds + (st * WAVES + wave) * 32;
    const f32x4 kz = *reinterpret_cast<const f32x4*>(rec + half * 4);
    const f32x4 vz = *reinterpret_cast<const f32x4*>(rec + 8 + half * 4);
    const int own = 2 * (lane & 3) + half;
    const float ksv = rec[16 + own], vsv = rec[24 + own];
    float x[4];
#pragma unroll
    for (int i = 0; i < 4; ++i) {
      const float cs[4] = {bflo(t.c[i][0]), bfhi(t.c[i][0]), bflo(t.c[i][1]), bfhi(t.c[i][1])};
      const float sn[4] = {bflo(t.s[i][0]), bfhi(t.s[i][0]), bflo(t.s[i][1]), bfhi(t.s[i][1])};
      float d = 0.f;
#pragma unroll
      for (int j = 0; j < 4; ++j) {
        const float w = qa[j] * cs[j] + qb[j] * sn[j];
        d = fmaf((float)t.k[i][j] - kz[i], w, d);
      }
      x[i] = d;
    }
    const bool b0 = lane & 1, b1 = lane & 2;
    const float t0 = x[0] + dpp<XOR1>(x[0]), t1 = x[1] + dpp<XOR1>(x[1]);
    const float t2 = x[2] + dpp<XOR1>(x[2]), t3 = x[3] + dpp<XOR1>(x[3]);
    const float y0 = b0 ? t1 : t0, y1 = b0 ? t3 : t2;
    const float u0 = y0 + dpp<XOR2>(y0), u1 = y1 + dpp<XOR2>(y1);
    float z = b1 ? u1 : u0;
    z += dpp<ROR4>(z);
    z += dpp<ROR8>(z);
    z = x16_sum(z);
    const float sc2 = z * ksv;
    float pm = fmaxf(sc2, dpp<XOR1>(sc2));
    pm = fmaxf(pm, dpp<XOR2>(pm));
    const float mn = fmaxf(m, pm);
    const float alpha = __builtin_amdgcn_exp2f(m - mn);
    const float p = __builtin_amdgcn_exp2f(sc2 - mn);
    m = mn;
    l = fmaf(l, alpha, p);
    const float pv = p * vsv;
    const float pi[4] = {dpp<0x00>(pv), dpp<0x55>(pv), dpp<0xAA>(pv), dpp<0xFF>(pv)};
#pragma unroll
    for (int j = 0; j < 4; ++j) acc[j] *= alpha;
#pragma unroll
    for (int i = 0; i < 4; ++i)
#pragma unroll
      for (int j = 0; j < 4; ++j) acc[j] = fmaf(pi[i], (float)t.v[i][j] - vz[i], acc[j]);
  };

#pragma unroll 1
  for (int st = 0; st < NSTEP - 2; st += 2) {
    compute(A, st);
    load(A, st + 2);
    __builtin_amdgcn_sched_barrier(0);
    compute(B, st + 1);
    load(B, st + 3);
    __builtin_amdgcn_sched_barrier(0);
  }
  compute(A, NSTEP - 2);
  compute(B, NSTEP - 1);

  l += dpp<XOR1>(l);
  l += dpp<XOR2>(l);
  {
    float* rec = l_mrg + (wave * 2 + half) * (D + 4);
    *reinterpret_cast<f32x4*>(rec + 4 + 4 * c) = f32x4{acc[0], acc[1], acc[2], acc[3]};
    if (c == 0) { rec[0] = m; rec[1] = l; }
  }
  __syncthreads();
  if (wave == 3) return;

  gu64* gran = (gu64*)(ws + WS_GRAN) + (size_t)bh * WPH * GREC;
  float M = -INFINITY, L = 0.f, o = 0.f;
  const int d = tid & (D - 1);
  {
#pragma unroll
    for (int w = 0; w < WAVES * 2; ++w) M = fmaxf(M, l_mrg[w * (D + 4)]);
#pragma unroll
    for (int w = 0; w < WAVES * 2; ++w) {
      const float* r = l_mrg + w * (D + 4);
      const float wt = __builtin_amdgcn_exp2f(r[0] - M);
      L = fmaf(wt, r[1], L);
      o = fmaf(wt, r[4 + d], o);
    }
  }
  if (mem != WPH - 1) {
    if (tid < REC) {
      const float val = tid < D ? o : (tid == D ? M : L);
      __hip_atomic_store(gran + mem * GREC + tid, ((unsigned long long)tag << 32) | __float_as_uint(val),
                         __ATOMIC_RELAXED, __HIP_MEMORY_SCOPE_AGENT);
    }
    STAMP_END();
    return;
  }
  if (wave == 2) return;
  float po[WPH - 1];
  float pml;
  {
    const int mlj = min(lane, 2 * (WPH - 1) - 1);
    gu64* ml_p = gran + (mlj >> 1) * GREC + D + (mlj & 1);
    unsigned spins = 0;
    while (true) {
      unsigned long long x0[WPH - 1];
      const unsigned long long x1 = __hip_atomic_load(ml_p, __ATOMIC_RELAXED, __HIP_MEMORY_SCOPE_AGENT);
#pragma unroll
      for (int j = 0; j < WPH - 1; ++j)
        x0[j] = __hip_atomic_load(gran + j * GREC + d, __ATOMIC_RELAXED, __HIP_MEMORY_SCOPE_AGENT);
      bool ok = (unsigned)(x1 >> 32) == tag;
      pml = __uint_as_float((unsigned)x1);
#pragma unroll
      for (int j = 0; j < WPH - 1; ++j) {
        po[j] = __uint_as_float((unsigned)x0[j]);
        ok &= (unsigned)(x0[j] >> 32) == tag;
      }
      if (__all(ok)) break;
      if (++spins > (1u << 22)) break;
    }
  }
  float pM[WPH - 1], pL[WPH - 1];
#pragma unroll
  for (int j = 0; j < WPH - 1; ++j) {
    pM[j] = __builtin_bit_cast(float, __builtin_amdgcn_readlane(__builtin_bit_cast(int, pml), 2 * j));
    pL[j] = __builtin_bit_cast(float, __builtin_amdgcn_readlane(__builtin_bit_cast(int, pml), 2 * j + 1));
  }
  float MM = M;
#pragma unroll
  for (int j = 0; j < WPH - 1; ++j) MM = fmaxf(MM, pM[j]);
  const float w0 = __builtin_amdgcn_exp2f(M - MM);
  float LL = w0 * L, oo = w0 * o;
#pragma unroll
  for (int j = 0; j < WPH - 1; ++j) {
    const float wt = __builtin_amdgcn_exp2f(pM[j] - MM);
    LL = fmaf(wt, pL[j], LL);
    oo = fmaf(wt, po[j], oo);
  }
  out[bh * D + d] = __float2bfloat16(oo / LL);
  if (tid == 0) __hip_atomic_store(epoch_p, tag, __ATOMIC_RELAXED, __HIP_MEMORY_SCOPE_AGENT);
  STAMP_END();
}

}

extern "C" void kernel_launch(void* const* d_in, const int* in_sizes, int n_in, void* d_out, int out_size,
                              void* d_ws, size_t ws_size, hipStream_t stream) {
  const unsigned short* q = (const unsigned short*)d_in[0];
  const int* qk = (const int*)d_in[1];
  const unsigned short* ksc = (const unsigned short*)d_in[2];
  const unsigned short* kzp = (const unsigned short*)d_in[3];
  const int* qv = (const int*)d_in[4];
  const unsigned short* vsc = (const unsigned short*)d_in[5];
  const unsigned short* vzp = (const unsigned short*)d_in[6];
  const unsigned short* qcos = (const unsigned short*)d_in[7];
  const unsigned short* qsin = (const unsigned short*)d_in[8];
  const unsigned short* kcos = (const unsigned short*)d_in[9];
  const unsigned short* ksin = (const unsigned short*)d_in[10];
  attn_decode<<<BH * WPH, NTHR, 0, stream>>>(q, qk, ksc, kzp, qv, vsc, vzp, qcos, qsin, kcos, ksin,
                                             (unsigned char*)d_ws, (__hip_bfloat16*)d_out);
}
```

```cpp
#include <hip/hip_runtime.h>
#include <hip/hip_bf16.h>
#include <stdint.h>

namespace {

constexpr int BH = 64;
constexpr int S = 4096;
constexpr int D = 128;
constexpr int WAVES = 4;
constexpr int NTHR = WAVES * 64;
constexpr int WPH = 8;
constexpr int TILE = 8;
constexpr int NSTEP = S / (WPH * WAVES * TILE);
constexpr int NT = NSTEP * WAVES;
constexpr int REC = D + 2;
constexpr size_t WS_EPOCH = 0;
constexpr size_t WS_GRAN = 64 * 1024;
constexpr int GREC = 136;
constexpr int AUX_NT = 2;

typedef int i32x4 __attribute__((ext_vector_type(4)));
typedef unsigned u32x2 __attribute__((ext_vector_type(2)));
typedef unsigned u32x4 __attribute__((ext_vector_type(4)));
typedef float f32x4 __attribute__((ext_vector_type(4)));
typedef __attribute__((address_space(1))) unsigned long long gu64;
typedef __attribute__((address_space(1))) unsigned gu32;

__device__ __forceinline__ float bflo(unsigned x) { return __uint_as_float(x << 16); }
__device__ __forceinline__ float bfhi(unsigned x) { return __uint_as_float(x & 0xffff0000u); }

template <int CTRL>
__device__ __forceinline__ float dpp(float x) {
  return __builtin_bit_cast(float, __builtin_amdgcn_mov_dpp(__builtin_bit_cast(int, x), CTRL, 0xf, 0xf, true));
}
constexpr int XOR1 = 0xB1, XOR2 = 0x4E, ROR4 = 0x124, ROR8 = 0x128;

__device__ __forceinline__ float x16_sum(float x) {
  auto s = __builtin_amdgcn_permlane16_swap(__float_as_uint(x), __float_as_uint(x), false, false);
  return __uint_as_float(s[0]) + __uint_as_float(s[1]);
}

struct Tile {
  i32x4 k[4];
  i32x4 v[4];
  u32x2 c[4];
  u32x2 s[4];
};

__device__ __forceinline__ int tile_row(int st_rot, int mem, int part) {
  return (st_rot >> 1) * 512 + mem * 64 + (st_rot & 1) * 32 + part * TILE;
}

__global__ void __launch_bounds__(NTHR, 2)
attn_decode(const unsigned short* __restrict__ q, const int* __restrict__ qk, const unsigned short* __restrict__ ksc,
            const unsigned short* __restrict__ kzp, const int* __restrict__ qv, const unsigned short* __restrict__ vsc,
            const unsigned short* __restrict__ vzp, const unsigned short* __restrict__ qcos,
            const unsigned short* __restrict__ qsin, const unsigned short* __restrict__ kcos,
            const unsigned short* __restrict__ ksin, unsigned char* __restrict__ ws,
            __hip_bfloat16* __restrict__ out) {
  __shared__ __attribute__((aligned(16))) float lds[NT * 32 + WAVES * 2 * (D + 4)];
  float* l_mrg = lds + NT * 32;

  const int bh = blockIdx.x / WPH, mem = blockIdx.x % WPH;
  const int tid = threadIdx.x, lane = tid & 63, wave = __builtin_amdgcn_readfirstlane(tid >> 6);
  const int half = lane >> 5, c = lane & 31;
  const int rot = bh & 15;

  const auto rk = __builtin_amdgcn_make_buffer_rsrc((void*)(qk + (size_t)bh * S * D), 0, S * D * 4, 0x00020000);
  const auto rv = __builtin_amdgcn_make_buffer_rsrc((void*)(qv + (size_t)bh * S * D), 0, S * D * 4, 0x00020000);
  const auto rc = __builtin_amdgcn_make_buffer_rsrc((void*)kcos, 0, S * D * 2, 0x00020000);
  const auto rs = __builtin_amdgcn_make_buffer_rsrc((void*)ksin, 0, S * D * 2, 0x00020000);
  const int dA = 4 * c, dB = (4 * c + 64) & 127;
  const unsigned voK = half * 512 + c * 16;
  const unsigned voC = half * 256 + dA * 2;
  const unsigned voS = half * 256 + dB * 2;

  auto load = [&](Tile& t, int st) {
    const int row0 = tile_row((st + rot) & 15, mem, (wave + st) & 3);
#pragma unroll
    for (int i = 0; i < 4; ++i)
      t.k[i] = __builtin_bit_cast(i32x4, __builtin_amdgcn_raw_buffer_load_b128(rk, voK + i * 1024, row0 * 512, AUX_NT));
#pragma unroll
    for (int i = 0; i < 4; ++i) {
      t.c[i] = __builtin_bit_cast(u32x2, __builtin_amdgcn_raw_buffer_load_b64(rc, voC + i * 512, row0 * 256, 0));
      t.s[i] = __builtin_bit_cast(u32x2, __builtin_amdgcn_raw_buffer_load_b64(rs, voS + i * 512, row0 * 256, 0));
    }
#pragma unroll
    for (int i = 0; i < 4; ++i)
      t.v[i] = __builtin_bit_cast(i32x4, __builtin_amdgcn_raw_buffer_load_b128(rv, voK + i * 1024, row0 * 512, AUX_NT));
  };

  Tile A, B;
  load(A, 0);
  __builtin_amdgcn_sched_barrier(0);

  u32x4 scl;
  {
    const unsigned short* s01 = (wave & 1) ? vzp : kzp;
    const unsigned short* s23 = (wave & 1) ? vsc : ksc;
    const unsigned short* sp = (wave & 2) ? s23 : s01;
    const auto rsc = __builtin_amdgcn_make_buffer_rsrc((void*)(sp + (size_t)bh * S), 0, S * 2, 0x00020000);
    const int row = tile_row(((lane >> 2) + rot) & 15, mem, lane & 3);
    scl = __builtin_bit_cast(u32x4, __builtin_amdgcn_raw_buffer_load_b128(rsc, row * 2, 0, AUX_NT));
  }
  const auto rq = __builtin_amdgcn_make_buffer_rsrc((void*)(q + bh * D), 0, D * 2, 0x00020000);
  const auto rqc = __builtin_amdgcn_make_buffer_rsrc((void*)qcos, 0, D * 2, 0x00020000);
  const auto rqs = __builtin_amdgcn_make_buffer_rsrc((void*)qsin, 0, D * 2, 0x00020000);
  const u32x2 qA = __builtin_bit_cast(u32x2, __builtin_amdgcn_raw_buffer_load_b64(rq, dA * 2, 0, 0));
  const u32x2 qB = __builtin_bit_cast(u32x2, __builtin_amdgcn_raw_buffer_load_b64(rq, dB * 2, 0, 0));
  const u32x2 cA = __builtin_bit_cast(u32x2, __builtin_amdgcn_raw_buffer_load_b64(rqc, dA * 2, 0, 0));
  const u32x2 cB = __builtin_bit_cast(u32x2, __builtin_amdgcn_raw_buffer_load_b64(rqc, dB * 2, 0, 0));
  const u32x2 sA = __builtin_bit_cast(u32x2, __builtin_amdgcn_raw_buffer_load_b64(rqs, dA * 2, 0, 0));
  const u32x2 sB = __builtin_bit_cast(u32x2, __builtin_amdgcn_raw_buffer_load_b64(rqs, dB * 2, 0, 0));
  __builtin_amdgcn_sched_barrier(0);
  load(B, 1);
  __builtin_amdgcn_sched_barrier(0);
  gu32* epoch_p = (gu32*)(ws + WS_EPOCH) + bh * 64;
  const unsigned tag = __hip_atomic_load(epoch_p, __ATOMIC_RELAXED, __HIP_MEMORY_SCOPE_AGENT) + 1u;

  {
    constexpr float SC = 0.088388347648318440f * 1.4426950408889634f;
    float* rec = lds + lane * 32 + wave * 8;
    const float mul = wave == 2 ? SC : 1.f;
    const float f[8] = {bflo(scl[0]) * mul, bfhi(scl[0]) * mul, bflo(scl[1]) * mul, bfhi(scl[1]) * mul,
                        bflo(scl[2]) * mul, bfhi(scl[2]) * mul, bflo(scl[3]) * mul, bfhi(scl[3]) * mul};
    const bool z = wave < 2;
    *reinterpret_cast<f32x4*>(rec) = f32x4{f[0], z ? f[2] : f[1], z ? f[4] : f[2], z ? f[6] : f[3]};
    *reinterpret_cast<f32x4*>(rec + 4) = f32x4{z ? f[1] : f[4], z ? f[3] : f[5], z ? f[5] : f[6], f[7]};
  }
  float qa[4], qb[4];
  {
    const float sgnA = (c < 16) ? -1.f : 1.f;
    const float q_a[4] = {bflo(qA[0]), bfhi(qA[0]), bflo(qA[1]), bfhi(qA[1])};
    const float q_b[4] = {bflo(qB[0]), bfhi(qB[0]), bflo(qB[1]), bfhi(qB[1])};
    const float c_a[4] = {bflo(cA[0]), bfhi(cA[0]), bflo(cA[1]), bfhi(cA[1])};
    const float c_b[4] = {bflo(cB[0]), bfhi(cB[0]), bflo(cB[1]), bfhi(cB[1])};
    const float s_a[4] = {bflo(sA[0]), bfhi(sA[0]), bflo(sA[1]), bfhi(sA[1])};
    const float s_b[4] = {bflo(sB[0]), bfhi(sB[0]), bflo(sB[1]), bfhi(sB[1])};
#pragma unroll
    for (int j = 0; j < 4; ++j) {
      const float qrA = q_a[j] * c_a[j] + sgnA * q_b[j] * s_a[j];
      const float qrB = q_b[j] * c_b[j] - sgnA * q_a[j] * s_b[j];
      qa[j] = qrA;
      qb[j] = -sgnA * qrB;
    }
  }
  __syncthreads();

  float m = -INFINITY, l = 0.f;
  float acc[4] = {0.f, 0.f, 0.f, 0.f};

  auto compute = [&](const Tile& t, int st) {
    const float* rec = lds + (st * WAVES + ((wave + st) & 3)) * 32;
    const f32x4 kz = *reinterpret_cast<const f32x4*>(rec + half * 4);
    const f32x4 vz = *reinterpret_cast<const f32x4*>(rec + 8 + half * 4);
    const int own = 2 * (lane & 3) + half;
    const float ksv = rec[16 + own], vsv = rec[24 + own];
    float x[4];
#pragma unroll
    for (int i = 0; i < 4; ++i) {
      const float cs[4] = {bflo(t.c[i][0]), bfhi(t.c[i][0]), bflo(t.c[i][1]), bfhi(t.c[i][1])};
      const float sn[4] = {bflo(t.s[i][0]), bfhi(t.s[i][0]), bflo(t.s[i][1]), bfhi(t.s[i][1])};
      float d = 0.f;
#pragma unroll
      for (int j = 0; j < 4; ++j) {
        const float w = qa[j] * cs[j] + qb[j] * sn[j];
        d = fmaf((float)t.k[i][j] - kz[i], w, d);
      }
      x[i] = d;
    }
    const bool b0 = lane & 1, b1 = lane & 2;
    const float t0 = x[0] + dpp<XOR1>(x[0]), t1 = x[1] + dpp<XOR1>(x[1]);
    const float t2 = x[2] + dpp<XOR1>(x[2]), t3 = x[3] + dpp<XOR1>(x[3]);
    const float y0 = b0 ? t1 : t0, y1 = b0 ? t3 : t2;
    const float u0 = y0 + dpp<XOR2>(y0), u1 = y1 + dpp<XOR2>(y1);
    float z = b1 ? u1 : u0;
    z += dpp<ROR4>(z);
    z += dpp<ROR8>(z);
    z = x16_sum(z);
    const float sc2 = z * ksv;
    float pm = fmaxf(sc2, dpp<XOR1>(sc2));
    pm = fmaxf(pm, dpp<XOR2>(pm));
    const float mn = fmaxf(m, pm);
    const float alpha = __builtin_amdgcn_exp2f(m - mn);
    const float p = __builtin_amdgcn_exp2f(sc2 - mn);
    m = mn;
    l = fmaf(l, alpha, p);
    const float pv = p * vsv;
    const float pi[4] = {dpp<0x00>(pv), dpp<0x55>(pv), dpp<0xAA>(pv), dpp<0xFF>(pv)};
#pragma unroll
    for (int j = 0; j < 4; ++j) acc[j] *= alpha;
#pragma unroll
    for (int i = 0; i < 4; ++i)
#pragma unroll
      for (int j = 0; j < 4; ++j) acc[j] = fmaf(pi[i], (float)t.v[i][j] - vz[i], acc[j]);
  };

#pragma unroll 1
  for (int st = 0; st < NSTEP - 2; st += 2) {
    compute(A, st);
    load(A, st + 2);
    __builtin_amdgcn_sched_barrier(0);
    compute(B, st + 1);
    load(B, st + 3);
    __builtin_amdgcn_sched_barrier(0);
  }
  compute(A, NSTEP - 2);
  compute(B, NSTEP - 1);

  l += dpp<XOR1>(l);
  l += dpp<XOR2>(l);
  {
    float* rec = l_mrg + (wave * 2 + half) * (D + 4);
    *reinterpret_cast<f32x4*>(rec + 4 + 4 * c) = f32x4{acc[0], acc[1], acc[2], acc[3]};
    if (c == 0) { rec[0] = m; rec[1] = l; }
  }
  __syncthreads();
  if (wave == 3) return;

  gu64* gran = (gu64*)(ws + WS_GRAN) + (size_t)bh * WPH * GREC;
  float M = -INFINITY, L = 0.f, o = 0.f;
  const int d = tid & (D - 1);
  {
#pragma unroll
    for (int w = 0; w < WAVES * 2; ++w) M = fmaxf(M, l_mrg[w * (D + 4)]);
#pragma unroll
    for (int w = 0; w < WAVES * 2; ++w) {
      const float* r = l_mrg + w * (D + 4);
      const float wt = __builtin_amdgcn_exp2f(r[0] - M);
      L = fmaf(wt, r[1], L);
      o = fmaf(wt, r[4 + d], o);
    }
  }
  if (mem != WPH - 1) {
    if (tid < REC) {
      const float val = tid < D ? o : (tid == D ? M : L);
      __hip_atomic_store(gran + mem * GREC + tid, ((unsigned long long)tag << 32) | __float_as_uint(val),
                         __ATOMIC_RELAXED, __HIP_MEMORY_SCOPE_AGENT);
    }
    return;
  }
  if (wave == 2) return;
  float po[WPH - 1];
  float pml;
  {
    const int mlj = min(lane, 2 * (WPH - 1) - 1);
    gu64* ml_p = gran + (mlj >> 1) * GREC + D + (mlj & 1);
    unsigned spins = 0;
    while (true) {
      unsigned long long x0[WPH - 1];
      const unsigned long long x1 = __hip_atomic_load(ml_p, __ATOMIC_RELAXED, __HIP_MEMORY_SCOPE_AGENT);
#pragma unroll
      for (int j = 0; j < WPH - 1; ++j)
        x0[j] = __hip_atomic_load(gran + j * GREC + d, __ATOMIC_RELAXED, __HIP_MEMORY_SCOPE_AGENT);
      bool ok = (unsigned)(x1 >> 32) == tag;
      pml = __uint_as_float((unsigned)x1);
#pragma unroll
      for (int j = 0; j < WPH - 1; ++j) {
        po[j] = __uint_as_float((unsigned)x0[j]);
        ok &= (unsigned)(x0[j] >> 32) == tag;
      }
      if (__all(ok)) break;
      if (++spins > (1u << 22)) break;
    }
  }
  float pM[WPH - 1], pL[WPH - 1];
#pragma unroll
  for (int j = 0; j < WPH - 1; ++j) {
    pM[j] = __builtin_bit_cast(float, __builtin_amdgcn_readlane(__builtin_bit_cast(int, pml), 2 * j));
    pL[j] = __builtin_bit_cast(float, __builtin_amdgcn_readlane(__builtin_bit_cast(int, pml), 2 * j + 1));
  }
  float MM = M;
#pragma unroll
  for (int j = 0; j < WPH - 1; ++j) MM = fmaxf(MM, pM[j]);
  const float w0 = __builtin_amdgcn_exp2f(M - MM);
  float LL = w0 * L, oo = w0 * o;
#pragma unroll
  for (int j = 0; j < WPH - 1; ++j) {
    const float wt = __builtin_amdgcn_exp2f(pM[j] - MM);
    LL = fmaf(wt, pL[j], LL);
    oo = fmaf(wt, po[j], oo);
  }
  out[bh * D + d] = __float2bfloat16(oo / LL);
  if (tid == 0) __hip_atomic_store(epoch_p, tag, __ATOMIC_RELAXED, __HIP_MEMORY_SCOPE_AGENT);
}

}

extern "C" void kernel_launch(void* const* d_in, const int* in_sizes, int n_in, void* d_out, int out_size,
                              void* d_ws, size_t ws_size, hipStream_t stream) {
  const unsigned short* q = (const unsigned short*)d_in[0];
  const int* qk = (const int*)d_in[1];
  const unsigned short* ksc = (const unsigned short*)d_in[2];
  const unsigned short* kzp = (const unsigned short*)d_in[3];
  const int* qv = (const int*)d_in[4];
  const unsigned short* vsc = (const unsigned short*)d_in[5];
  const unsigned short* vzp = (const unsigned short*)d_in[6];
  const unsigned short* qcos = (const unsigned short*)d_in[7];
  const unsigned short* qsin = (const unsigned short*)d_in[8];
  const unsigned short* kcos = (const unsigned short*)d_in[9];
  const unsigned short* ksin = (const unsigned short*)d_in[10];
  attn_decode<<<BH * WPH, NTHR, 0, stream>>>(q, qk, ksc, kzp, qv, vsc, vzp, qcos, qsin, kcos, ksin,
                                             (unsigned char*)d_ws, (__hip_bfloat16*)d_out);
}
```
